```python
import torch

def _softmax(x, axis=-1):
    return torch.softmax(x, dim=axis)
def _gelu(x):
    return torch.nn.functional.gelu(x, approximate="tanh")
def _silu(x):
    return torch.nn.functional.silu(x)
def _sigmoid(x):
    return torch.sigmoid(x)
def _layernorm(x, axis=-1, eps=1e-5):
    m = x.mean(dim=axis, keepdim=True)
    v = x.var(dim=axis, keepdim=True, unbiased=False)
    return (x - m) / torch.sqrt(v + eps)
def _scatter_add(tgt, idx, src, axis=0):
    return tgt.clone().index_add_(axis, idx, src)
def _segment_sum(src, seg, n):
    out = torch.zeros((n,) + src.shape[1:], dtype=src.dtype)
    return out.index_add_(0, seg, src)
def _pad(x, pw):
    flat = tuple(v for pair in reversed(pw) for v in pair)
    return torch.nn.functional.pad(x, flat)

kernel_name = 'sequential_preLN__softmax_full_mha__glu_gated_reglu__prompt'

def setup_inputs():

    _w = (lambda a: (lambda t: t.to(torch.bfloat16) if t.is_floating_point() else t)(torch.as_tensor(a)))
    def _A_setup():
        B, S, D = 1, 2048, 512
        return {
            "x": _w((lambda *s: torch.randn(*s, dtype=torch.bfloat16) * 0.05)(B, S, D)),
            "wqkv": _w((lambda *s: torch.randn(*s, dtype=torch.bfloat16) * 0.05)(D, 3 * D)),
            "wo": _w((lambda *s: torch.randn(*s, dtype=torch.bfloat16) * 0.05)(D, D)),
        }
    _A_in = _A_setup()

    def _B_setup():
        B, S, D, F = 1, 2048, 512, 1792
        return {
            "x": _w((lambda *s: torch.randn(*s, dtype=torch.bfloat16) * 0.05)(B, S, D)),
            "W_gate": _w((lambda *s: torch.randn(*s, dtype=torch.bfloat16) * 0.4)(D, F) * (2.0 / D) ** 0.5),
            "W_up": _w((lambda *s: torch.randn(*s, dtype=torch.bfloat16) * 0.4)(D, F) * (2.0 / D) ** 0.5),
            "W_down": _w((lambda *s: torch.randn(*s, dtype=torch.bfloat16) * 0.4)(F, D) * (1.0 / F) ** 0.5),
        }
    _B_in = _B_setup()

    return {
        "A_x": _A_in["x"],
        "A_wqkv": _A_in["wqkv"],
        "A_wo": _A_in["wo"],
        "B_x": _B_in["x"],
        "B_W_gate": _B_in["W_gate"],
        "B_W_up": _B_in["W_up"],
        "B_W_down": _B_in["W_down"],
    }

def reference(A_x, A_wqkv, A_wo, B_x, B_W_gate, B_W_up, B_W_down):
    def _A_ref(x, wqkv, wo):
        B, S, D = x.shape
        H = 32; Dh = D // H
        qkv = (lambda x, *d: x.permute(*d))((x @ wqkv).reshape(B, S, 3, H, Dh), 2, 0, 3, 1, 4)
        q, k, v = qkv[0], qkv[1], qkv[2]
        scale = Dh ** -0.5
        scores = torch.einsum("bhqd,bhkd->bhqk", q, k) * scale
        p = _softmax(scores, axis=-1)
        o = torch.einsum("bhqk,bhkd->bhqd", p, v)
        return (lambda x, *d: x.permute(*d))(o, 0, 2, 1, 3).reshape(B, S, D) @ wo

    def _B_ref(x, W_gate, W_up, W_down):
        gate = torch.einsum("bsd,df->bsf", x, W_gate)
        up = torch.einsum("bsd,df->bsf", x, W_up)
        gate = gate * (lambda x, dt: x.to(dt))(gate > 0, gate.dtype)
        h = gate * up
        return torch.einsum("bsf,fd->bsd", h, W_down)

    x = A_x
    h = x + _A_ref(_layernorm(x, axis=-1), A_wqkv, A_wo)
    h2 = h + _B_ref(_layernorm(h, axis=-1), B_W_gate, B_W_up, B_W_down)
    return h2
```

```cpp
#include <hip/hip_runtime.h>
#include <hip/hip_bf16.h>
#include <stdint.h>

typedef unsigned short bf16_t;
__device__ __forceinline__ float bf2f(bf16_t v) { return __uint_as_float(((unsigned)v) << 16); }
__device__ __forceinline__ bf16_t f2bf(float f) {
  unsigned u = __float_as_uint(f);
  unsigned r = u + 0x7FFFu + ((u >> 16) & 1u);
  return (bf16_t)(r >> 16);
}
#define S_ 2048
#define D_ 512
#define F_ 1792
#define H_ 32
#define DH 16

__global__ void ln_kernel(const bf16_t* __restrict__ x, bf16_t* __restrict__ y) {
  int row = blockIdx.x; int t = threadIdx.x;
  float v[8]; float s = 0.f;
  for (int i = 0; i < 8; ++i) { v[i] = bf2f(x[row * D_ + t * 8 + i]); s += v[i]; }
  for (int o = 32; o > 0; o >>= 1) s += __shfl_xor(s, o);
  float mean = s / D_;
  float q = 0.f;
  for (int i = 0; i < 8; ++i) { float d = v[i] - mean; q += d * d; }
  for (int o = 32; o > 0; o >>= 1) q += __shfl_xor(q, o);
  float var = q / D_;
  float mb = bf2f(f2bf(mean)), vb = bf2f(f2bf(var));
  float den = bf2f(f2bf(sqrtf(bf2f(f2bf(vb + 1e-5f)))));
  for (int i = 0; i < 8; ++i) { float d = bf2f(f2bf(v[i] - mb)); y[row * D_ + t * 8 + i] = f2bf(d / den); }
}
__global__ void gemm_naive(const bf16_t* __restrict__ A, const bf16_t* __restrict__ B, bf16_t* __restrict__ C, int M, int N, int K, int mode, const bf16_t* __restrict__ R) {
  int n = blockIdx.x * 64 + threadIdx.x, m = blockIdx.y;
  if (n >= N) return;
  float acc = 0.f;
  for (int k = 0; k < K; ++k) acc += bf2f(A[(size_t)m * K + k]) * bf2f(B[(size_t)k * N + n]);
  bf16_t r = f2bf(acc);
  if (mode == 1) r = f2bf(bf2f(R[(size_t)m * N + n]) + bf2f(r));
  C[(size_t)m * N + n] = r;
}
__global__ void attn_naive(const bf16_t* __restrict__ qkv, bf16_t* __restrict__ o) {
  __shared__ float sc[S_];
  __shared__ float red[256];
  int q = blockIdx.x, h = blockIdx.y, t = threadIdx.x;
  float qv[DH];
  for (int d = 0; d < DH; ++d) qv[d] = bf2f(qkv[(size_t)q * 1536 + h * DH + d]);
  float mx = -INFINITY;
  for (int k = t; k < S_; k += 256) {
    float a = 0.f;
    for (int d = 0; d < DH; ++d) a += qv[d] * bf2f(qkv[(size_t)k * 1536 + 512 + h * DH + d]);
    float s = bf2f(f2bf(bf2f(f2bf(a)) * 0.25f));
    sc[k] = s; mx = fmaxf(mx, s);
  }
  red[t] = mx; __syncthreads();
  for (int o2 = 128; o2 > 0; o2 >>= 1) { if (t < o2) red[t] = fmaxf(red[t], red[t + o2]); __syncthreads(); }
  mx = red[0]; __syncthreads();
  float sum = 0.f;
  for (int k = t; k < S_; k += 256) { float e = __expf(sc[k] - mx); sc[k] = e; sum += e; }
  red[t] = sum; __syncthreads();
  for (int o2 = 128; o2 > 0; o2 >>= 1) { if (t < o2) red[t] += red[t + o2]; __syncthreads(); }
  sum = red[0]; __syncthreads();
  if (t < DH) {
    float a = 0.f;
    for (int k = 0; k < S_; ++k) a += bf2f(f2bf(sc[k] / sum)) * bf2f(qkv[(size_t)k * 1536 + 1024 + h * DH + t]);
    o[(size_t)q * D_ + h * DH + t] = f2bf(a);
  }
}
__global__ void reglu(const bf16_t* g, const bf16_t* u, bf16_t* a, int n) {
  int i = blockIdx.x * 256 + threadIdx.x; if (i >= n) return;
  float gg = bf2f(g[i]); gg = gg > 0.f ? gg : 0.f;
  a[i] = f2bf(gg * bf2f(u[i]));
}
extern "C" void kernel_launch(void* const* d_in, const int* in_sizes, int n_in, void* d_out, int out_size, void* d_ws, size_t ws_size, hipStream_t stream) {
  const bf16_t* x = (const bf16_t*)d_in[0];
  const bf16_t* wqkv = (const bf16_t*)d_in[1];
  const bf16_t* wo = (const bf16_t*)d_in[2];
  const bf16_t* wg = (const bf16_t*)d_in[4];
  const bf16_t* wu = (const bf16_t*)d_in[5];
  const bf16_t* wd = (const bf16_t*)d_in[6];
  bf16_t* out = (bf16_t*)d_out;
  bf16_t* ws = (bf16_t*)d_ws;
  bf16_t* xn = ws; bf16_t* qkv = xn + S_ * D_; bf16_t* o = qkv + S_ * 1536; bf16_t* h = o + S_ * D_;
  bf16_t* hn = h + S_ * D_; bf16_t* g = hn + S_ * D_; bf16_t* u = g + S_ * F_; bf16_t* a = u + S_ * F_;
  ln_kernel<<<S_, 64, 0, stream>>>(x, xn);
  gemm_naive<<<dim3(1536 / 64, S_), 64, 0, stream>>>(xn, wqkv, qkv, S_, 1536, D_, 0, nullptr);
  attn_naive<<<dim3(S_, H_), 256, 0, stream>>>(qkv, o);
  gemm_naive<<<dim3(D_ / 64, S_), 64, 0, stream>>>(o, wo, h, S_, D_, D_, 1, x);
  ln_kernel<<<S_, 64, 0, stream>>>(h, hn);
  gemm_naive<<<dim3(F_ / 64, S_), 64, 0, stream>>>(hn, wg, g, S_, F_, D_, 0, nullptr);
  gemm_naive<<<dim3(F_ / 64, S_), 64, 0, stream>>>(hn, wu, u, S_, F_, D_, 0, nullptr);
  reglu<<<(S_ * F_ + 255) / 256, 256, 0, stream>>>(g, u, a, S_ * F_);
  gemm_naive<<<dim3(D_ / 64, S_), 64, 0, stream>>>(a, wd, out, S_, D_, F_, 1, h);
}
```

```cpp
#include <hip/hip_runtime.h>
#include <hip/hip_bf16.h>
#include <stdint.h>

typedef unsigned short u16;
typedef __attribute__((ext_vector_type(8))) short bf16x8;
typedef __attribute__((ext_vector_type(4))) short bf16x4;
typedef __attribute__((ext_vector_type(4))) float f32x4;
typedef __attribute__((ext_vector_type(16))) float f32x16;
typedef __attribute__((ext_vector_type(4))) unsigned u32x4;
typedef __attribute__((ext_vector_type(2))) unsigned u32x2;
#define LDS_AS __attribute__((address_space(3)))

#define SEQ 2048
#define DM 512
#define FF 1792
#define NH 32
#define HD 16

__device__ __forceinline__ float bf2f(u16 v) { return __uint_as_float(((unsigned)v) << 16); }
__device__ __forceinline__ u16 f2bf(float f) {
  unsigned u = __float_as_uint(f);
  unsigned r = u + 0x7FFFu + ((u >> 16) & 1u);
  return (u16)(r >> 16);
}
__device__ __forceinline__ float rbf(float f) { return bf2f(f2bf(f)); }
__device__ __forceinline__ unsigned cvtpk(float lo, float hi) {
  unsigned r; asm volatile("v_cvt_pk_bf16_f32 %0, %1, %2" : "=v"(r) : "v"(lo), "v"(hi)); return r;
}

__global__ __launch_bounds__(256) void ln_kernel(const u16* __restrict__ x, u16* __restrict__ y) {
  int row = blockIdx.x * 4 + (threadIdx.x >> 6), t = threadIdx.x & 63;
  u32x4 raw = *(const u32x4*)(x + (size_t)row * DM + t * 8);
  float v[8];
#pragma unroll
  for (int i = 0; i < 4; ++i) { v[2 * i] = __uint_as_float(raw[i] << 16); v[2 * i + 1] = __uint_as_float(raw[i] & 0xffff0000u); }
  float s = 0.f;
#pragma unroll
  for (int i = 0; i < 8; ++i) s += v[i];
#pragma unroll
  for (int o = 32; o > 0; o >>= 1) s += __shfl_xor(s, o);
  float mean = s * (1.0f / DM);
  float q = 0.f;
#pragma unroll
  for (int i = 0; i < 8; ++i) { float d = v[i] - mean; q += d * d; }
#pragma unroll
  for (int o = 32; o > 0; o >>= 1) q += __shfl_xor(q, o);
  float var = q * (1.0f / DM);
  float mb = rbf(mean), vb = rbf(var);
  float den = rbf(sqrtf(rbf(vb + 1e-5f)));
  u32x4 o4;
#pragma unroll
  for (int i = 0; i < 4; ++i) {
    float a = rbf(v[2 * i] - mb) / den, b = rbf(v[2 * i + 1] - mb) / den;
    o4[i] = (unsigned)f2bf(a) | ((unsigned)f2bf(b) << 16);
  }
  *(u32x4*)(y + (size_t)row * DM + t * 8) = o4;
}

enum { EPI_QKV = 0, EPI_RES = 1, EPI_REGLU = 2 };

template <int BN> __device__ __forceinline__ int bswz(int k) {
  if (BN >= 128) return (k & 3) | (((k >> 3) & 1) << 2);
  else return ((k >> 1) & 1) | (((k >> 3) & 1) << 1);
}

template <int N> __device__ __forceinline__ void wait_vm() {
  if (N == 0) asm volatile("s_waitcnt vmcnt(0)" ::: "memory");
  else if (N == 4) asm volatile("s_waitcnt vmcnt(4)" ::: "memory");
  else if (N == 6) asm volatile("s_waitcnt vmcnt(6)" ::: "memory");
  else if (N == 8) asm volatile("s_waitcnt vmcnt(8)" ::: "memory");
  else if (N == 12) asm volatile("s_waitcnt vmcnt(12)" ::: "memory");
  else if (N == 16) asm volatile("s_waitcnt vmcnt(16)" ::: "memory");
  else if (N == 20) asm volatile("s_waitcnt vmcnt(20)" ::: "memory");
  else if (N == 24) asm volatile("s_waitcnt vmcnt(24)" ::: "memory");
  else if (N == 28) asm volatile("s_waitcnt vmcnt(28)" ::: "memory");
  else asm volatile("s_waitcnt vmcnt(0)" ::: "memory");
}

template <int BM, int BN, int WM, int WN, int KS, int MODE, bool SWAP, int NS, int BK>
__device__ __forceinline__ void gemm_main(const u16* __restrict__ A, int lda, const u16* __restrict__ B, const u16* __restrict__ B2, int ldb,
                                          int K, int m0, int n0, unsigned char* smem, f32x4 (&acc)[BM / WM / 16][BN / WN / 16]) {
  constexpr int NT = WM * WN * KS * 64, MT = BM / WM / 16, NTL = BN / WN / 16;
  constexpr int ACR = BK / 8, ACH = BM * ACR / NT, CPR = BN / 8, BCH = BK * CPR / NT, L = ACH + BCH;
  constexpr int ABYTES = BM * BK * 2, BBYTES = BK * BN * 2, STAGE = ABYTES + BBYTES, AMSK = ACR - 1;
  const int tid = threadIdx.x, lane = tid & 63, wid = tid >> 6, wk = wid / (WM * WN), wm = (wid % (WM * WN)) / WN, wn = wid % WN;
  const int fr = lane & 15, fq = lane >> 4;
  const u16* asrc[ACH]; const u16* bsrc[BCH];
#pragma unroll
  for (int i = 0; i < ACH; ++i) { int c = i * NT + tid, row = c / ACR, pos = c % ACR; asrc[i] = A + (size_t)(m0 + row) * lda + ((pos ^ (row & AMSK)) * 8); }
#pragma unroll
  for (int i = 0; i < BCH; ++i) {
    int c = i * NT + tid, k = c / CPR, pos = c % CPR, unit = pos >> 1, half = pos & 1, u2 = unit ^ bswz<BN>(k), col = (u2 * 2 + half) * 8;
    if (MODE == 0) bsrc[i] = B + (size_t)k * ldb + n0 + col;
    else { int j = col >> 4; bsrc[i] = ((j & 1) ? B2 : B) + (size_t)k * ldb + (n0 >> 1) + (j >> 1) * 16 + (col & 15); }
  }
  int nk = K / BK; asm volatile("" : "+s"(nk));
  auto stage = [&](int kt, int buf) {
    unsigned char* sa = smem + buf * STAGE; unsigned char* sb = sa + ABYTES;
#pragma unroll
    for (int i = 0; i < ACH; ++i)
      __builtin_amdgcn_global_load_lds((const unsigned*)(asrc[i] + kt * BK), (unsigned*)(sa + (i * NT + tid) * 16), 16, 0, 0);
#pragma unroll
    for (int i = 0; i < BCH; ++i)
      __builtin_amdgcn_global_load_lds((const unsigned*)(bsrc[i] + (size_t)kt * BK * ldb), (unsigned*)(sb + (i * NT + tid) * 16), 16, 0, 0);
  };
#pragma unroll
  for (int i = 0; i < MT; ++i)
#pragma unroll
    for (int j = 0; j < NTL; ++j) acc[i][j] = (f32x4){0.f, 0.f, 0.f, 0.f};
#pragma unroll
  for (int s = 0; s < NS - 1; ++s) if (s < nk) stage(s, s);
  const int q4 = (lane & 15) >> 2, p4 = lane & 3;
  const int fsw = bswz<BN>(8 * fq + q4);
  int buf = 0;
  for (int kt = 0; kt < nk; ++kt) {
    if (kt + NS - 2 < nk) wait_vm<(NS - 2) * L>();
    else wait_vm<0>();
    __builtin_amdgcn_s_barrier();
    asm volatile("" ::: "memory");
    { int nb = buf + NS - 1; if (nb >= NS) nb -= NS; if (kt + NS - 1 < nk) stage(kt + NS - 1, nb); }
    const unsigned char* sa = smem + buf * STAGE; const unsigned char* sb = sa + ABYTES;
    constexpr int KQ = BK / 32 / KS;
    bf16x8 a[KQ][MT], bb[KQ][NTL];
#pragma unroll
    for (int kq = 0; kq < KQ; ++kq) {
      const int kk = KS == 2 ? wk * (BK / 64) + kq : kq;
#pragma unroll
      for (int j = 0; j < NTL; ++j) {
        int nt = wn * NTL + j, k = kk * 32 + 8 * fq + q4;
        const unsigned char* p = sb + k * (BN * 2) + ((nt ^ fsw) * 32) + p4 * 8;
        bf16x4 lo = __builtin_bit_cast(bf16x4, __builtin_amdgcn_ds_read_tr16_b64_v4i16((LDS_AS bf16x4*)p));
        bf16x4 hi = __builtin_bit_cast(bf16x4, __builtin_amdgcn_ds_read_tr16_b64_v4i16((LDS_AS bf16x4*)(p + 4 * BN * 2)));
        bb[kq][j] = (bf16x8){lo[0], lo[1], lo[2], lo[3], hi[0], hi[1], hi[2], hi[3]};
      }
#pragma unroll
      for (int i = 0; i < MT; ++i) { int row = wm * (BM / WM) + i * 16 + fr; a[kq][i] = *(const bf16x8*)(sa + row * (BK * 2) + (((kk * 4 + fq) ^ (row & AMSK)) * 16)); }
    }
#pragma unroll
    for (int kq = 0; kq < KQ; ++kq)
#pragma unroll
      for (int i = 0; i < MT; ++i)
#pragma unroll
        for (int j = 0; j < NTL; ++j) {
          if (SWAP) acc[i][j] = __builtin_amdgcn_mfma_f32_16x16x32_bf16(bb[kq][j], a[kq][i], acc[i][j], 0, 0, 0);
          else acc[i][j] = __builtin_amdgcn_mfma_f32_16x16x32_bf16(a[kq][i], bb[kq][j], acc[i][j], 0, 0, 0);
        }
    asm volatile("" ::: "memory");
    buf = buf + 1 == NS ? 0 : buf + 1;
  }
  if (KS == 2) {
    __syncthreads();
    f32x4* red = (f32x4*)smem;
    const int w4 = wid % (WM * WN);
    if (wk == 1) {
#pragma unroll
      for (int i = 0; i < MT; ++i)
#pragma unroll
        for (int j = 0; j < NTL; ++j) red[(w4 * MT * NTL + i * NTL + j) * 64 + lane] = acc[i][j];
    }
    __syncthreads();
    if (wk == 0) {
#pragma unroll
      for (int i = 0; i < MT; ++i)
#pragma unroll
        for (int j = 0; j < NTL; ++j) acc[i][j] += red[(w4 * MT * NTL + i * NTL + j) * 64 + lane];
    }
  }
}

__global__ __launch_bounds__(512) void gemm_qkv(const u16* __restrict__ A, const u16* __restrict__ W, u16* __restrict__ Qh, u16* __restrict__ Kh, u16* __restrict__ Vt) {
  extern __shared__ __attribute__((aligned(16))) unsigned char smem[];
  constexpr int BM = 128, BN = 128;
  const int xcd = blockIdx.x & 7, idx = blockIdx.x >> 3;
  const int bn = idx % 12, bm = xcd * 2 + idx / 12, m0 = bm * BM, n0 = bn * BN;
  const int lane = threadIdx.x & 63, wid = threadIdx.x >> 6, wm = wid / 4, wn = wid % 4, fr = lane & 15, fq = lane >> 4;
  f32x4 acc[4][2];
  const int region = n0 >> 9;
  if (region < 2) {
    gemm_main<BM, BN, 2, 4, 1, 0, true, 2, 128>(A, DM, W, nullptr, 3 * DM, DM, m0, n0, smem, acc);
    u16* dst = region == 0 ? Qh : Kh;
#pragma unroll
    for (int i = 0; i < 4; ++i)
#pragma unroll
      for (int j = 0; j < 2; ++j) {
        int tok = m0 + wm * 64 + i * 16 + fr, col = (n0 & 511) + wn * 32 + j * 16, head = col >> 4;
        u32x2 w = {cvtpk(acc[i][j][0], acc[i][j][1]), cvtpk(acc[i][j][2], acc[i][j][3])};
        *(u32x2*)(dst + ((size_t)head * SEQ + tok) * HD + 4 * fq) = w;
      }
  } else {
    gemm_main<BM, BN, 2, 4, 1, 0, false, 2, 128>(A, DM, W, nullptr, 3 * DM, DM, m0, n0, smem, acc);
#pragma unroll
    for (int i = 0; i < 4; ++i)
#pragma unroll
      for (int j = 0; j < 2; ++j) {
        int tok0 = m0 + wm * 64 + i * 16, col = (n0 & 511) + wn * 32 + j * 16, head = col >> 4;
        int t = tok0 >> 5, s2 = (tok0 >> 4) & 1;
        u32x2 w = {cvtpk(acc[i][j][0], acc[i][j][1]), cvtpk(acc[i][j][2], acc[i][j][3])};
        *(u32x2*)(Vt + (size_t)head * SEQ * HD + ((((t * 2 + s2) * 16 + fr) * 2 + (fq & 1)) * 8 + 4 * (fq >> 1))) = w;
      }
  }
}

__global__ __launch_bounds__(512) void gemm_res(const u16* __restrict__ A, int K, const u16* __restrict__ W, const u16* __restrict__ res, u16* __restrict__ out) {
  extern __shared__ __attribute__((aligned(16))) unsigned char smem[];
  constexpr int BM = 64, BN = 64;
  const int xcd = blockIdx.x & 7, idx = blockIdx.x >> 3;
  const int bn = idx & 7, bm = xcd * 4 + (idx >> 3), m0 = bm * BM, n0 = bn * BN;
  const int lane = threadIdx.x & 63, wid = threadIdx.x >> 6, wm = wid / 2, wn = wid % 2, fr = lane & 15, fq = lane >> 4;
  f32x4 acc[2][2];
  gemm_main<BM, BN, 2, 2, 2, 0, true, 4, 128>(A, K, W, nullptr, DM, K, m0, n0, smem, acc);
  if (threadIdx.x >= 256) return;
#pragma unroll
  for (int i = 0; i < 2; ++i)
#pragma unroll
    for (int j = 0; j < 2; ++j) {
      int row = m0 + wm * 32 + i * 16 + fr, col = n0 + wn * 32 + j * 16 + 4 * fq;
      u32x2 r2 = *(const u32x2*)(res + (size_t)row * DM + col);
      float r0 = __uint_as_float(r2[0] << 16), r1 = __uint_as_float(r2[0] & 0xffff0000u), r2f = __uint_as_float(r2[1] << 16), r3 = __uint_as_float(r2[1] & 0xffff0000u);
      u32x2 w = {cvtpk(r0 + rbf(acc[i][j][0]), r1 + rbf(acc[i][j][1])), cvtpk(r2f + rbf(acc[i][j][2]), r3 + rbf(acc[i][j][3]))};
      *(u32x2*)(out + (size_t)row * DM + col) = w;
    }
}

__global__ __launch_bounds__(512) void gemm_glu(const u16* __restrict__ A, const u16* __restrict__ Wg, const u16* __restrict__ Wu, u16* __restrict__ act) {
  extern __shared__ __attribute__((aligned(16))) unsigned char smem[];
  constexpr int BM = 128, BN = 256;
  const int xcd = blockIdx.x & 7, idx = blockIdx.x >> 3;
  const int bn = idx % 14, bm = xcd * 2 + idx / 14, m0 = bm * BM, n0 = bn * BN;
  const int lane = threadIdx.x & 63, wid = threadIdx.x >> 6, wm = wid / 4, wn = wid % 4, fr = lane & 15, fq = lane >> 4;
  f32x4 acc[4][4];
  gemm_main<BM, BN, 2, 4, 1, 1, true, 3, 64>(A, DM, Wg, Wu, FF, DM, m0, n0, smem, acc);
#pragma unroll
  for (int i = 0; i < 4; ++i)
#pragma unroll
    for (int j = 0; j < 2; ++j) {
      int row = m0 + wm * 64 + i * 16 + fr, f = (n0 >> 1) + (wn * 2 + j) * 16 + 4 * fq;
      float o[4];
#pragma unroll
      for (int e = 0; e < 4; ++e) { float g = rbf(acc[i][2 * j][e]), u = rbf(acc[i][2 * j + 1][e]); g = g > 0.f ? g : 0.f; o[e] = g * u; }
      u32x2 w = {cvtpk(o[0], o[1]), cvtpk(o[2], o[3])};
      *(u32x2*)(act + (size_t)row * FF + f) = w;
    }
}

#define ATT_LDS (131072 + 4096)
__global__ __launch_bounds__(1024) void attn_kernel(const u16* __restrict__ Qh, const u16* __restrict__ Kh, const u16* __restrict__ Vt, u16* __restrict__ O) {
  extern __shared__ __attribute__((aligned(16))) unsigned char smem[];
  const int bid = blockIdx.x, head = (bid & 7) * 4 + ((bid >> 3) & 3), qblk = bid >> 5;
  const int tid = threadIdx.x, lane = tid & 63, w = tid >> 6, qt = w & 7, kh = w >> 3;
  const int r = lane & 31, hh = lane >> 5;
  const u16* Kg = Kh + (size_t)head * SEQ * HD; const u16* Vg = Vt + (size_t)head * SEQ * HD;
#pragma unroll
  for (int i = 0; i < 4; ++i)
    __builtin_amdgcn_global_load_lds((const unsigned*)(Kg + (i * 1024 + tid) * 8), (unsigned*)(smem + (i * 1024 + tid) * 16), 16, 0, 0);
#pragma unroll
  for (int i = 0; i < 4; ++i)
    __builtin_amdgcn_global_load_lds((const unsigned*)(Vg + (i * 1024 + tid) * 8), (unsigned*)(smem + 65536 + (i * 1024 + tid) * 16), 16, 0, 0);
  if (tid < 256) { unsigned c = (((tid & 31) >> 1) == 0) ? 0x3F803F80u : 0u; *(u32x4*)(smem + 131072 + tid * 16) = (u32x4){c, c, c, c}; }
  const int q0 = qblk * 256 + qt * 32;
  bf16x8 qraw = *(const bf16x8*)(Qh + ((size_t)head * SEQ + q0 + r) * HD + 8 * hh);
  asm volatile("s_waitcnt vmcnt(0)" ::: "memory");
  __syncthreads();
  bf16x8 qf;
  { const float C = 0.25f * 1.4426950408889634f;
    u32x4 qq = __builtin_bit_cast(u32x4, qraw), qs;
#pragma unroll
    for (int i = 0; i < 4; ++i) qs[i] = cvtpk(__uint_as_float(qq[i] << 16) * C, __uint_as_float(qq[i] & 0xffff0000u) * C);
    qf = __builtin_bit_cast(bf16x8, qs); }
  const unsigned kaddr0 = kh * 32768 + r * 32 + hh * 16;
  const unsigned vaddr0 = (r < 16) ? 65536 + kh * 32768 + r * 32 + hh * 16 : 131072 + (r - 16) * 32 + hh * 16;
  const unsigned vinc = (r < 16) ? 4096 : 0;
  f32x16 o = {0};
  float m;
  {
    bf16x8 kf = *(const bf16x8*)(smem + kaddr0);
    f32x16 s = {0};
    s = __builtin_amdgcn_mfma_f32_32x32x16_bf16(kf, qf, s, 0, 0, 0);
    float mx = s[0];
#pragma unroll
    for (int i = 1; i < 16; ++i) mx = fmaxf(mx, s[i]);
    { auto rr = __builtin_amdgcn_permlane32_swap(__float_as_uint(mx), __float_as_uint(mx), false, false);
      mx = fmaxf(__uint_as_float(rr[0]), __uint_as_float(rr[1])); }
    m = mx;
    f32x16 negm;
#pragma unroll
    for (int i = 0; i < 16; ++i) negm[i] = -mx;
    unsigned kaddr = kaddr0, vaddr = vaddr0;
    for (int tt = 0; tt < 32; tt += 4) {
#pragma unroll
      for (int u = 0; u < 4; ++u) {
        kf = *(const bf16x8*)(smem + kaddr + u * 1024);
        bf16x8 v0 = *(const bf16x8*)(smem + vaddr + u * 1024);
        bf16x8 v1 = *(const bf16x8*)(smem + vaddr + u * 1024 + 512);
        s = __builtin_amdgcn_mfma_f32_32x32x16_bf16(kf, qf, negm, 0, 0, 0);
#pragma unroll
        for (int i = 0; i < 16; ++i) s[i] = __builtin_amdgcn_exp2f(s[i]);
        u32x4 p0 = {cvtpk(s[0], s[1]), cvtpk(s[2], s[3]), cvtpk(s[4], s[5]), cvtpk(s[6], s[7])};
        u32x4 p1 = {cvtpk(s[8], s[9]), cvtpk(s[10], s[11]), cvtpk(s[12], s[13]), cvtpk(s[14], s[15])};
        o = __builtin_amdgcn_mfma_f32_32x32x16_bf16(v0, __builtin_bit_cast(bf16x8, p0), o, 0, 0, 0);
        o = __builtin_amdgcn_mfma_f32_32x32x16_bf16(v1, __builtin_bit_cast(bf16x8, p1), o, 0, 0, 0);
      }
      kaddr += 4096; vaddr += vinc;
    }
  }
  {
    float chk = 0.f;
#pragma unroll
    for (int i = 0; i < 9; ++i) chk += fabsf(o[i]);
    if (__any(!(chk < 1.0e30f))) {
      o = (f32x16){0}; m = -INFINITY;
      unsigned kaddr = kaddr0, vaddr = vaddr0;
      for (int tt = 0; tt < 32; ++tt) {
        bf16x8 kf = *(const bf16x8*)(smem + kaddr);
        bf16x8 v0 = *(const bf16x8*)(smem + vaddr + (tt & 3) * 1024);
        bf16x8 v1 = *(const bf16x8*)(smem + vaddr + (tt & 3) * 1024 + 512);
        f32x16 s = {0};
        s = __builtin_amdgcn_mfma_f32_32x32x16_bf16(kf, qf, s, 0, 0, 0);
        float mx = s[0];
#pragma unroll
        for (int i = 1; i < 16; ++i) mx = fmaxf(mx, s[i]);
        { auto rr = __builtin_amdgcn_permlane32_swap(__float_as_uint(mx), __float_as_uint(mx), false, false);
          mx = fmaxf(__uint_as_float(rr[0]), __uint_as_float(rr[1])); }
        float mn = fmaxf(m, mx);
        float alpha = (m == -INFINITY) ? 0.f : __builtin_amdgcn_exp2f(m - mn);
        m = mn;
#pragma unroll
        for (int i = 0; i < 16; ++i) s[i] = __builtin_amdgcn_exp2f(s[i] - mn);
#pragma unroll
        for (int i = 0; i < 9; ++i) o[i] *= alpha;
        u32x4 p0 = {cvtpk(s[0], s[1]), cvtpk(s[2], s[3]), cvtpk(s[4], s[5]), cvtpk(s[6], s[7])};
        u32x4 p1 = {cvtpk(s[8], s[9]), cvtpk(s[10], s[11]), cvtpk(s[12], s[13]), cvtpk(s[14], s[15])};
        o = __builtin_amdgcn_mfma_f32_32x32x16_bf16(v0, __builtin_bit_cast(bf16x8, p0), o, 0, 0, 0);
        o = __builtin_amdgcn_mfma_f32_32x32x16_bf16(v1, __builtin_bit_cast(bf16x8, p1), o, 0, 0, 0);
        kaddr += 1024; if ((tt & 3) == 3) vaddr += vinc;
      }
    }
  }
  __syncthreads();
  float* xch = (float*)smem;
  if (kh == 1) {
#pragma unroll
    for (int i = 0; i < 9; ++i) xch[(qt * 10 + i) * 64 + lane] = o[i];
    xch[(qt * 10 + 9) * 64 + lane] = m;
  }
  __syncthreads();
  if (kh == 0) {
    float m1 = xch[(qt * 10 + 9) * 64 + lane];
    float mn = fmaxf(m, m1);
    float a0 = __builtin_amdgcn_exp2f(m - mn), a1 = __builtin_amdgcn_exp2f(m1 - mn);
#pragma unroll
    for (int i = 0; i < 9; ++i) o[i] = o[i] * a0 + xch[(qt * 10 + i) * 64 + lane] * a1;
    float l = o[8];
    { auto rr = __builtin_amdgcn_permlane32_swap(__float_as_uint(l), __float_as_uint(l), false, false);
      l = __uint_as_float(rr[0]); }
    l = hh ? l : o[8];
    float inv = 1.0f / l;
    u16* dst = O + (size_t)(q0 + r) * DM + head * HD + 4 * hh;
    u32x2 w0 = {cvtpk(o[0] * inv, o[1] * inv), cvtpk(o[2] * inv, o[3] * inv)};
    u32x2 w1 = {cvtpk(o[4] * inv, o[5] * inv), cvtpk(o[6] * inv, o[7] * inv)};
    *(u32x2*)dst = w0; *(u32x2*)(dst + 8) = w1;
  }
}

extern "C" void kernel_launch(void* const* d_in, const int* in_sizes, int n_in, void* d_out, int out_size, void* d_ws, size_t ws_size, hipStream_t stream) {
  const u16* x = (const u16*)d_in[0];
  const u16* wqkv = (const u16*)d_in[1];
  const u16* wo = (const u16*)d_in[2];
  const u16* wg = (const u16*)d_in[4];
  const u16* wu = (const u16*)d_in[5];
  const u16* wd = (const u16*)d_in[6];
  u16* out = (u16*)d_out;
  u16* ws = (u16*)d_ws;
  u16* xn = ws;
  u16* Qh = xn + SEQ * DM;
  u16* Kh = Qh + SEQ * DM;
  u16* Vt = Kh + SEQ * DM;
  u16* Oa = Vt + SEQ * DM;
  u16* h = Oa + SEQ * DM;
  u16* hn = h + SEQ * DM;
  u16* act = hn + SEQ * DM;
  constexpr int G128 = 4 * (128 * 128 + 64 * 128 * 2), G64 = 8 * (64 * 128 + 64 * 64 * 2);
  (void)hipFuncSetAttribute((const void*)gemm_qkv, hipFuncAttributeMaxDynamicSharedMemorySize, G128);
  constexpr int GGLU = 3 * (128 * 64 * 2 + 64 * 256 * 2);
  (void)hipFuncSetAttribute((const void*)gemm_glu, hipFuncAttributeMaxDynamicSharedMemorySize, GGLU);
  (void)hipFuncSetAttribute((const void*)gemm_res, hipFuncAttributeMaxDynamicSharedMemorySize, G64);
  (void)hipFuncSetAttribute((const void*)attn_kernel, hipFuncAttributeMaxDynamicSharedMemorySize, ATT_LDS);
  ln_kernel<<<SEQ / 4, 256, 0, stream>>>(x, xn);
  gemm_qkv<<<16 * 12, 512, G128, stream>>>(xn, wqkv, Qh, Kh, Vt);
  attn_kernel<<<256, 1024, ATT_LDS, stream>>>(Qh, Kh, Vt, Oa);
  gemm_res<<<32 * 8, 512, G64, stream>>>(Oa, DM, wo, x, h);
  ln_kernel<<<SEQ / 4, 256, 0, stream>>>(h, hn);
  gemm_glu<<<16 * 14, 512, GGLU, stream>>>(hn, wg, wu, act);
  gemm_res<<<32 * 8, 512, G64, stream>>>(act, FF, wd, h, out);
}
```
